```python
import torch

def _softmax(x, axis=-1):
    return torch.softmax(x, dim=axis)
def _gelu(x):
    return torch.nn.functional.gelu(x, approximate="tanh")
def _silu(x):
    return torch.nn.functional.silu(x)
def _sigmoid(x):
    return torch.sigmoid(x)
def _layernorm(x, axis=-1, eps=1e-5):
    m = x.mean(dim=axis, keepdim=True)
    v = x.var(dim=axis, keepdim=True, unbiased=False)
    return (x - m) / torch.sqrt(v + eps)
def _scatter_add(tgt, idx, src, axis=0):
    return tgt.clone().index_add_(axis, idx, src)
def _segment_sum(src, seg, n):
    out = torch.zeros((n,) + src.shape[1:], dtype=src.dtype)
    return out.index_add_(0, seg, src)
def _pad(x, pw):
    flat = tuple(v for pair in reversed(pw) for v in pair)
    return torch.nn.functional.pad(x, flat)

kernel_name = 'gather_scatter__cheap_equivariant_egnn__graph'

def setup_inputs():

    _w = (lambda a: (lambda t: t.to(torch.bfloat16) if t.is_floating_point() else t)(torch.as_tensor(a)))
    N, deg, D, De = 10000, 64, 64, max(16, 1)
    E = N * deg
    return {
        "h": _w((lambda *s: torch.randn(*s, dtype=torch.bfloat16) * 0.1)(N, D)),
        "pos": _w((lambda *s: torch.randn(*s, dtype=torch.bfloat16) * 0.8)(N, 3)),
        "e_attr": _w((lambda *s: torch.randn(*s, dtype=torch.bfloat16) * 0.8)(E, De)),
        "src": _w((lambda lo, hi, *s: torch.randint(lo, hi, s))(0, N, E).to(torch.int64)),
        "dst": _w((lambda lo, hi, *s: torch.randint(lo, hi, s))(0, N, E).to(torch.int64)),
        "w_e": _w(0.1 * (lambda *s: torch.randn(*s, dtype=torch.bfloat16) * 0.8)(2 * D + 1 + De, D)),
        "w_x": _w(0.1 * (lambda *s: torch.randn(*s, dtype=torch.bfloat16) * 0.1)(D, 1)),
        "w_h": _w(0.1 * (lambda *s: torch.randn(*s, dtype=torch.bfloat16) * 0.1)(2 * D, D)),
    }

def reference(h, pos, e_attr, src, dst, w_e, w_x, w_h):
    N, D = h.shape[0], h.shape[1]
    diff = pos[dst] - pos[src]
    dist2 = torch.sum(diff * diff, axis=-1, keepdims=True)
    e_in = torch.concatenate([h[dst], h[src], dist2, e_attr], axis=-1)
    m_ij = _silu(e_in @ w_e)
    coord_scale = m_ij @ w_x
    pos_new = pos + _segment_sum(diff * coord_scale, dst, N)
    m_i = _segment_sum(m_ij, dst, N)
    h_new = h + _silu(torch.concatenate([h, m_i], axis=-1) @ w_h)
    return h_new, pos_new
```

```cpp
#include <hip/hip_runtime.h>
#include <hip/hip_bf16.h>
#include <stdint.h>


using bf16x8 = __attribute__((ext_vector_type(8))) short;
using f32x16 = __attribute__((ext_vector_type(16))) float;
using f32x2  = __attribute__((ext_vector_type(2))) float;
using bf16x2v = __attribute__((ext_vector_type(2))) __bf16;
using u32x4  = __attribute__((ext_vector_type(4))) unsigned;
typedef unsigned short u16;

#define DI __device__ __forceinline__
#define MFMA(a, b, c) __builtin_amdgcn_mfma_f32_32x32x16_bf16((a), (b), (c), 0, 0, 0)

constexpr int NPB = 7;
constexpr int NB  = 128;
constexpr int K1T = 1024;
constexpr int PER = 8;
constexpr int WIN = 512;
constexpr int APS = 80;
constexpr int NFRAG = 34;

DI unsigned pk2(float a, float b) {
  f32x2 v = {a, b};
  bf16x2v r = __builtin_convertvector(v, bf16x2v);
  return __builtin_bit_cast(unsigned, r);
}
DI float bf2f(unsigned bits16) { return __uint_as_float(bits16 << 16); }
DI unsigned f2bf(float a) { return pk2(a, 0.f) & 0xffffu; }
DI float rbf(float a) { return __uint_as_float(pk2(a, 0.f) << 16); }
DI float silu(float x) {
  float e = __builtin_amdgcn_exp2f(x * -1.4426950408889634f);
  return x * __builtin_amdgcn_rcpf(1.0f + e);
}
DI void wave_fence() {
  __builtin_amdgcn_fence(__ATOMIC_RELEASE, "wavefront");
  __builtin_amdgcn_wave_barrier();
  __builtin_amdgcn_fence(__ATOMIC_ACQUIRE, "wavefront");
}

__global__ __launch_bounds__(K1T) void k1_sort(const int* __restrict__ src, const int* __restrict__ dst,
                                               const u16* __restrict__ w_e, const u16* __restrict__ w_h,
                                               int E, int CH, int NBK, int TS,
                                               uint2* __restrict__ recs, u16* __restrict__ tab,
                                               u16* __restrict__ wfrag) {
  extern __shared__ __attribute__((aligned(16))) char smem[];
  __shared__ unsigned wsum[K1T / 64];
  const int tid = threadIdx.x, blk = blockIdx.x;
  if (blk >= NB) {
    for (int i = tid; i < NFRAG * 512; i += K1T) {
      const int f = i >> 9, lane = (i >> 3) & 63, j = i & 7, c = lane & 31, h = lane >> 5;
      u16 v;
      if (f < 8)       { const int s = f >> 1, t = f & 1;               v = w_e[(16 * s + 8 * h + j) * 64 + 32 * t + c]; }
      else if (f < 16) { const int s = (f - 8) >> 1, t = f & 1;         v = w_e[(64 + 16 * s + 8 * h + j) * 64 + 32 * t + c]; }
      else if (f < 18) { const int t = f & 1;                           v = w_e[(129 + 8 * h + j) * 64 + 32 * t + c]; }
      else             { const int s = (f - 18) >> 1, t = f & 1;        v = w_h[(16 * s + 8 * h + j) * 64 + 32 * t + c]; }
      wfrag[i] = v;
    }
    return;
  }
  unsigned* hist = (unsigned*)smem;
  uint2* lrec = (uint2*)(smem + (((NBK + 1) * 4 + 15) & ~15));
  const int e0 = blk * CH;
  const int n = max(0, min(E - e0, CH));
  for (int i = tid; i <= NBK; i += K1T) hist[i] = 0;
  int d[PER], s[PER];
#pragma unroll
  for (int k = 0; k < PER; ++k) {
    const int i = tid + k * K1T;
    d[k] = -1; s[k] = 0;
    if (i < n) { d[k] = dst[e0 + i]; s[k] = src[e0 + i]; }
  }
  __syncthreads();
  unsigned rk[PER]; int bk[PER];
#pragma unroll
  for (int k = 0; k < PER; ++k) {
    bk[k] = 0; rk[k] = 0;
    if (d[k] >= 0) { bk[k] = d[k] / NPB; rk[k] = atomicAdd(&hist[bk[k]], 1u); }
  }
  __syncthreads();
  const int q = (NBK + K1T - 1) / K1T;
  const int b0 = tid * q;
  unsigned loc = 0;
  for (int i = 0; i < q; ++i) { const int b = b0 + i; if (b < NBK) loc += hist[b]; }
  unsigned inc = loc;
#pragma unroll
  for (int o = 1; o < 64; o <<= 1) { unsigned v = __shfl_up(inc, o, 64); if ((tid & 63) >= o) inc += v; }
  if ((tid & 63) == 63) wsum[tid >> 6] = inc;
  __syncthreads();
  unsigned base = 0;
  for (int w = 0; w < (tid >> 6); ++w) base += wsum[w];
  unsigned run = base + inc - loc;
  for (int i = 0; i < q; ++i) {
    const int b = b0 + i;
    if (b < NBK) { const unsigned c = hist[b]; hist[b] = run; run += c; }
  }
  if (tid == 0) hist[NBK] = (unsigned)n;
  __syncthreads();
  for (int i = tid; i <= NBK; i += K1T) tab[(size_t)blk * TS + i] = (u16)hist[i];
#pragma unroll
  for (int k = 0; k < PER; ++k) {
    if (d[k] >= 0) {
      const unsigned dl = (unsigned)(d[k] - bk[k] * NPB);
      lrec[hist[bk[k]] + rk[k]] = make_uint2((unsigned)(e0 + tid + k * K1T), (unsigned)s[k] | (dl << 28));
    }
  }
  __syncthreads();
  for (int i = tid; i < n; i += K1T) recs[(size_t)e0 + i] = lrec[i];
}

struct EdgeBuf {
  u32x4 ea;
  u32x4 hs[4];
  unsigned p0, p1, p2;
  unsigned dl;
};

__global__ __launch_bounds__(NB, 2) void k2_main(const u16* __restrict__ hh, const u16* __restrict__ pos,
                                                 const u16* __restrict__ e_attr, const u16* __restrict__ w_e,
                                                 const u16* __restrict__ w_x,
                                                 int N, int CH, int TS,
                                                 const uint2* __restrict__ recs, const u16* __restrict__ tab,
                                                 const u32x4* __restrict__ wfrag,
                                                 u16* __restrict__ out_h, u16* __restrict__ out_p) {
  __shared__ __attribute__((aligned(16))) unsigned list[WIN];
  __shared__ __attribute__((aligned(16))) uint2 rl[WIN];
  __shared__ __attribute__((aligned(16))) char ap[2][32 * APS];
  __shared__ __attribute__((aligned(16))) float red[32 * 64];
  __shared__ __attribute__((aligned(16))) u16 mi[8 * 72];
  __shared__ __attribute__((aligned(16))) float posn[8 * 4];
  __shared__ unsigned wsum[2];

  const int tid = threadIdx.x, b = blockIdx.x;
  const int lane = tid & 63, wv = tid >> 6, r = lane & 31, h = lane >> 5;
  const int node0 = b * NPB;

  const unsigned lo = tab[(size_t)tid * TS + b], hi = tab[(size_t)tid * TS + b + 1];

  u32x4 hn[4];
  {
    const bool nv = (r < NPB) && (node0 + r < N);
    const u32x4* hp = (const u32x4*)(hh + (size_t)(nv ? node0 + r : 0) * 64 + 8 * h);
#pragma unroll
    for (int s = 0; s < 4; ++s) { u32x4 v = hp[2 * s]; if (!nv) v = (u32x4)(0u); hn[s] = v; }
  }
  if (tid < 32) {
    const int nd = tid >> 2, c = tid & 3;
    float v = 0.f;
    if (c < 3 && nd < NPB && node0 + nd < N) v = bf2f(pos[(size_t)(node0 + nd) * 3 + c]);
    posn[tid] = v;
  }
  {
    for (int i = tid; i < 2 * 32 * APS / 4; i += NB) ((unsigned*)ap)[i] = 0u;
  }
  u32x4 w2f[4][2], w3f[2], exf[2];
#pragma unroll
  for (int s = 0; s < 4; ++s) { w2f[s][0] = wfrag[(8 + 2 * s) * 64 + lane]; w2f[s][1] = wfrag[(9 + 2 * s) * 64 + lane]; }
  w3f[0] = wfrag[16 * 64 + lane]; w3f[1] = wfrag[17 * 64 + lane];
#pragma unroll
  for (int t = 0; t < 2; ++t) {
    f32x16 a = (f32x16)(0.f);
#pragma unroll
    for (int s = 0; s < 4; ++s) a = MFMA(__builtin_bit_cast(bf16x8, hn[s]), __builtin_bit_cast(bf16x8, wfrag[(2 * s + t) * 64 + lane]), a);
    float v[8];
#pragma unroll
    for (int i = 0; i < 4; ++i) {
      const float o = __shfl_xor(a[i], 32, 64);
      v[i] = h ? o : a[i];
      v[4 + i] = h ? a[i] : o;
    }
    unsigned e16[8];
#pragma unroll
    for (int j = 0; j < 7; ++j) {
      const unsigned hb = f2bf(v[j]);
      const unsigned lb = f2bf(v[j] - bf2f(hb));
      e16[j] = h ? lb : hb;
    }
    e16[7] = h ? 0u : (unsigned)w_e[128 * 64 + 32 * t + r];
    exf[t] = (u32x4){e16[0] | (e16[1] << 16), e16[2] | (e16[3] << 16), e16[4] | (e16[5] << 16), e16[6] | (e16[7] << 16)};
  }

  const unsigned cnt = hi - lo;
  unsigned inc = cnt;
#pragma unroll
  for (int o = 1; o < 64; o <<= 1) { unsigned v = __shfl_up(inc, o, 64); if (lane >= o) inc += v; }
  if (lane == 63) wsum[wv] = inc;
  __syncthreads();
  const unsigned pre = inc - cnt + (wv ? wsum[0] : 0u);
  const int total = (int)(wsum[0] + wsum[1]);
  const unsigned gsrc = (unsigned)(tid * CH) + lo;

  f32x16 Y[2];
  Y[0] = (f32x16)(0.f); Y[1] = (f32x16)(0.f);
  char* apw = ap[wv];
  const int pslot = ((r >> 4) << 4) + (((r >> 2) & 1) << 3) + (((r >> 3) & 1) << 2) + (r & 3);

  for (int w0 = 0; w0 < total; w0 += WIN) {
    if (w0) __syncthreads();
    {
      const int i0 = max(0, w0 - (int)pre), i1 = min((int)cnt, w0 + WIN - (int)pre);
      for (int i = i0; i < i1; ++i) list[(int)pre + i - w0] = gsrc + i;
    }
    __syncthreads();
    const int wn = min(WIN, total - w0);
#pragma unroll
    for (int k = 0; k < WIN / NB; ++k) {
      const int sl = tid + k * NB;
      if (sl < wn) rl[sl] = recs[list[sl]];
    }
    __syncthreads();
    const int nch = (wn + 31) >> 5;

    auto load_chunk = [&](int c, EdgeBuf& B) {
      const int sl = c * 32 + r;
      uint2 rec = make_uint2(0u, 7u << 28);
      if (sl < wn) rec = rl[sl];
      const unsigned e = rec.x, sidx = rec.y & 0x0fffffffu;
      B.dl = rec.y >> 28;
      B.ea = *(const u32x4*)(e_attr + (size_t)e * 16 + 8 * h);
      const u32x4* hp = (const u32x4*)(hh + (size_t)sidx * 64 + 8 * h);
#pragma unroll
      for (int s = 0; s < 4; ++s) B.hs[s] = hp[2 * s];
      const u16* pp = pos + (size_t)sidx * 3;
      B.p0 = pp[0]; B.p1 = pp[1]; B.p2 = pp[2];
    };

    EdgeBuf cur, nxt;
    if (wv < nch) load_chunk(wv, cur);
    for (int c = wv; c < nch; c += 2) {
      if (c + 2 < nch) load_chunk(c + 2, nxt);
      const unsigned dl = cur.dl;
      const float4 pd = *(const float4*)(posn + dl * 4);
      const float d0 = rbf(pd.x - bf2f(cur.p0)), d1 = rbf(pd.y - bf2f(cur.p1)), d2 = rbf(pd.z - bf2f(cur.p2));
      const float dist2 = rbf(d0 * d0) + rbf(d1 * d1) + rbf(d2 * d2);
      const unsigned oh = 0x3F80u << ((dl & 1u) << 4);
      const unsigned dq = dl >> 1;
      u32x4 ex;
      ex[0] = dq == 0 ? oh : 0u;
      ex[1] = dq == 1 ? oh : 0u;
      ex[2] = dq == 2 ? oh : 0u;
      ex[3] = (dl == 6 ? 0x3F80u : 0u) | (h ? 0u : (f2bf(dist2) << 16));
      u16* a0 = (u16*)(apw + (dl * 4 + 2 * h) * APS + pslot * 2);
      u16* a1 = (u16*)((char*)a0 + APS);
      if (dl < 7) {
        *a0 = (u16)(h ? (__float_as_uint(d1) >> 16) : 0x3F80u);
        *a1 = (u16)((h ? __float_as_uint(d2) : __float_as_uint(d0)) >> 16);
      }
      wave_fence();
      const u32x4 a2_0 = *(const u32x4*)(apw + r * APS + h * 16);
      const u32x4 a2_1 = *(const u32x4*)(apw + r * APS + 32 + h * 16);
      wave_fence();
      if (dl < 7) { *a0 = 0; *a1 = 0; }
#pragma unroll
      for (int t = 0; t < 2; ++t) {
        f32x16 x = (f32x16)(0.f);
        x = MFMA(__builtin_bit_cast(bf16x8, cur.ea), __builtin_bit_cast(bf16x8, w3f[t]), x);
        x = MFMA(__builtin_bit_cast(bf16x8, ex), __builtin_bit_cast(bf16x8, exf[t]), x);
#pragma unroll
        for (int s = 0; s < 4; ++s) x = MFMA(__builtin_bit_cast(bf16x8, cur.hs[s]), __builtin_bit_cast(bf16x8, w2f[s][t]), x);
        u32x4 xs0, xs1;
#pragma unroll
        for (int i = 0; i < 4; ++i) {
          xs0[i] = pk2(silu(x[2 * i]), silu(x[2 * i + 1]));
          xs1[i] = pk2(silu(x[8 + 2 * i]), silu(x[9 + 2 * i]));
        }
        Y[t] = MFMA(__builtin_bit_cast(bf16x8, a2_0), __builtin_bit_cast(bf16x8, xs0), Y[t]);
        Y[t] = MFMA(__builtin_bit_cast(bf16x8, a2_1), __builtin_bit_cast(bf16x8, xs1), Y[t]);
      }
      cur = nxt;
    }
  }

  if (wv == 1) {
#pragma unroll
    for (int t = 0; t < 2; ++t)
#pragma unroll
      for (int i = 0; i < 16; ++i) red[(t * 16 + i) * 64 + lane] = Y[t][i];
  }
  __syncthreads();
  if (wv == 1) return;
#pragma unroll
  for (int t = 0; t < 2; ++t)
#pragma unroll
    for (int i = 0; i < 16; ++i) Y[t][i] += red[(t * 16 + i) * 64 + lane];

  const float wx0 = bf2f(w_x[r]), wx1 = bf2f(w_x[32 + r]);
#pragma unroll
  for (int g = 0; g < 4; ++g) {
    const int nd = 2 * g + h;
    mi[nd * 72 + r] = (u16)f2bf(Y[0][4 * g]);
    mi[nd * 72 + 32 + r] = (u16)f2bf(Y[1][4 * g]);
#pragma unroll
    for (int k = 1; k < 4; ++k) {
      float p = wx0 * Y[0][4 * g + k] + wx1 * Y[1][4 * g + k];
#pragma unroll
      for (int o = 1; o < 32; o <<= 1) p += __shfl_xor(p, o, 64);
      if (r == 0 && nd < NPB && node0 + nd < N)
        out_p[(size_t)(node0 + nd) * 3 + (k - 1)] = (u16)f2bf(posn[nd * 4 + k - 1] + rbf(p));
    }
  }
  wave_fence();
  u32x4 mf[4];
#pragma unroll
  for (int s = 0; s < 4; ++s) {
    u32x4 v = *(const u32x4*)(mi + (r & 7) * 72 + 16 * s + 8 * h);
    if (r >= 8) v = (u32x4)(0u);
    mf[s] = v;
  }
#pragma unroll
  for (int t = 0; t < 2; ++t) {
    f32x16 a = (f32x16)(0.f);
#pragma unroll
    for (int s = 0; s < 4; ++s) a = MFMA(__builtin_bit_cast(bf16x8, hn[s]), __builtin_bit_cast(bf16x8, wfrag[(18 + 2 * s + t) * 64 + lane]), a);
#pragma unroll
    for (int s = 0; s < 4; ++s) a = MFMA(__builtin_bit_cast(bf16x8, mf[s]), __builtin_bit_cast(bf16x8, wfrag[(26 + 2 * s + t) * 64 + lane]), a);
#pragma unroll
    for (int i = 0; i < 4; ++i) {
      const int nd = 4 * h + i;
      if (nd < NPB && node0 + nd < N) {
        const size_t o = (size_t)(node0 + nd) * 64 + 32 * t + r;
        const float sv = rbf(silu(rbf(a[i])));
        out_h[o] = (u16)f2bf(bf2f(hh[o]) + sv);
      }
    }
  }
}

extern "C" void kernel_launch(void* const* d_in, const int* in_sizes, int n_in,
                              void* d_out, int out_size, void* d_ws, size_t ws_size,
                              hipStream_t stream) {
  const u16* h      = (const u16*)d_in[0];
  const u16* pos    = (const u16*)d_in[1];
  const u16* e_attr = (const u16*)d_in[2];
  const int* src    = (const int*)d_in[3];
  const int* dst    = (const int*)d_in[4];
  const u16* w_e    = (const u16*)d_in[5];
  const u16* w_x    = (const u16*)d_in[6];
  const u16* w_h    = (const u16*)d_in[7];
  const int N = in_sizes[0] / 64;
  const int E = in_sizes[3];
  const int CH = (E + NB - 1) / NB;
  const int NBK = (N + NPB - 1) / NPB;
  const int TS = (NBK + 1 + 7) & ~7;

  char* ws = (char*)d_ws;
  uint2* recs = (uint2*)ws;                                   size_t off = (size_t)NB * CH * 8;
  off = (off + 255) & ~(size_t)255;
  u16* tab = (u16*)(ws + off);                                off += (size_t)NB * TS * 2;
  off = (off + 255) & ~(size_t)255;
  u16* wfrag = (u16*)(ws + off);

  u16* out_h = (u16*)d_out;
  u16* out_p = out_h + (size_t)N * 64;

  const size_t lds1 = (((size_t)(NBK + 1) * 4 + 15) & ~(size_t)15) + (size_t)CH * 8;
  k1_sort<<<NB + 1, K1T, lds1, stream>>>(src, dst, w_e, w_h, E, CH, NBK, TS, recs, tab, wfrag);
  k2_main<<<NBK, NB, 0, stream>>>(h, pos, e_attr, w_e, w_x, N, CH, TS, recs, tab, (const u32x4*)wfrag, out_h, out_p);
}
```

```cpp
#include <hip/hip_runtime.h>
#include <hip/hip_bf16.h>
#include <stdint.h>

using bf16x8 = __attribute__((ext_vector_type(8))) short;
using f32x16 = __attribute__((ext_vector_type(16))) float;
using f32x2  = __attribute__((ext_vector_type(2))) float;
using bf16x2v = __attribute__((ext_vector_type(2))) __bf16;
using u32x4  = __attribute__((ext_vector_type(4))) unsigned;
typedef unsigned short u16;

#define NBUF 3
#define OCC 2
#define DI __device__ __forceinline__
#define MFMA(a, b, c) __builtin_amdgcn_mfma_f32_32x32x16_bf16((a), (b), (c), 0, 0, 0)

constexpr int NPB = 7;
constexpr int NB  = 128;
constexpr int K1T = 1024;
constexpr int PER = 8;
constexpr int WIN = 512;
constexpr int APS = 80;
constexpr int NFRAG = 34;

DI unsigned pk2(float a, float b) {
  f32x2 v = {a, b};
  bf16x2v r = __builtin_convertvector(v, bf16x2v);
  return __builtin_bit_cast(unsigned, r);
}
DI float bf2f(unsigned bits16) { return __uint_as_float(bits16 << 16); }
DI unsigned f2bf(float a) { return pk2(a, 0.f) & 0xffffu; }
DI float rbf(float a) { return __uint_as_float(pk2(a, 0.f) << 16); }
DI float silu(float x) {
  float e = __builtin_amdgcn_exp2f(x * -1.4426950408889634f);
  return x * __builtin_amdgcn_rcpf(1.0f + e);
}
DI void wave_fence() {
  __builtin_amdgcn_fence(__ATOMIC_RELEASE, "wavefront");
  __builtin_amdgcn_wave_barrier();
  __builtin_amdgcn_fence(__ATOMIC_ACQUIRE, "wavefront");
}

__global__ __launch_bounds__(K1T) void k1_sort(const int* __restrict__ src, const int* __restrict__ dst,
                                               const u16* __restrict__ w_e, const u16* __restrict__ w_h,
                                               int E, int CH, int NBK, int TS,
                                               uint2* __restrict__ recs, u16* __restrict__ tab,
                                               u16* __restrict__ wfrag) {
  extern __shared__ __attribute__((aligned(16))) char smem[];
  __shared__ unsigned wsum[K1T / 64];
  const int tid = threadIdx.x, blk = blockIdx.x;
  if (blk >= NB) {
    for (int i = tid; i < NFRAG * 512; i += K1T) {
      const int f = i >> 9, lane = (i >> 3) & 63, j = i & 7, c = lane & 31, h = lane >> 5;
      u16 v;
      if (f < 8)       { const int s = f >> 1, t = f & 1;               v = w_e[(16 * s + 8 * h + j) * 64 + 32 * t + c]; }
      else if (f < 16) { const int s = (f - 8) >> 1, t = f & 1;         v = w_e[(64 + 16 * s + 8 * h + j) * 64 + 32 * t + c]; }
      else if (f < 18) { const int t = f & 1;                           v = w_e[(129 + 8 * h + j) * 64 + 32 * t + c]; }
      else             { const int s = (f - 18) >> 1, t = f & 1;        v = w_h[(16 * s + 8 * h + j) * 64 + 32 * t + c]; }
      wfrag[i] = v;
    }
    return;
  }
  unsigned* hist = (unsigned*)smem;
  uint2* lrec = (uint2*)(smem + (((NBK + 1) * 4 + 15) & ~15));
  const int e0 = blk * CH;
  const int n = max(0, min(E - e0, CH));
  for (int i = tid; i <= NBK; i += K1T) hist[i] = 0;
  int d[PER], s[PER];
#pragma unroll
  for (int k = 0; k < PER; ++k) {
    const int i = tid + k * K1T;
    d[k] = -1; s[k] = 0;
    if (i < n) { d[k] = dst[e0 + i]; s[k] = src[e0 + i]; }
  }
  __syncthreads();
  unsigned rk[PER]; int bk[PER];
#pragma unroll
  for (int k = 0; k < PER; ++k) {
    bk[k] = 0; rk[k] = 0;
    if (d[k] >= 0) { bk[k] = d[k] / NPB; rk[k] = atomicAdd(&hist[bk[k]], 1u); }
  }
  __syncthreads();
  const int q = (NBK + K1T - 1) / K1T;
  const int b0 = tid * q;
  unsigned loc = 0;
  for (int i = 0; i < q; ++i) { const int b = b0 + i; if (b < NBK) loc += hist[b]; }
  unsigned inc = loc;
#pragma unroll
  for (int o = 1; o < 64; o <<= 1) { unsigned v = __shfl_up(inc, o, 64); if ((tid & 63) >= o) inc += v; }
  if ((tid & 63) == 63) wsum[tid >> 6] = inc;
  __syncthreads();
  unsigned base = 0;
  for (int w = 0; w < (tid >> 6); ++w) base += wsum[w];
  unsigned run = base + inc - loc;
  for (int i = 0; i < q; ++i) {
    const int b = b0 + i;
    if (b < NBK) { const unsigned c = hist[b]; hist[b] = run; run += c; }
  }
  if (tid == 0) hist[NBK] = (unsigned)n;
  __syncthreads();
  for (int i = tid; i <= NBK; i += K1T) tab[(size_t)blk * TS + i] = (u16)hist[i];
#pragma unroll
  for (int k = 0; k < PER; ++k) {
    if (d[k] >= 0) {
      const unsigned dl = (unsigned)(d[k] - bk[k] * NPB);
      lrec[hist[bk[k]] + rk[k]] = make_uint2((unsigned)(e0 + tid + k * K1T), (unsigned)s[k] | (dl << 28));
    }
  }
  __syncthreads();
  for (int i = tid; i < n; i += K1T) recs[(size_t)e0 + i] = lrec[i];
}

struct EdgeBuf {
  u32x4 ea;
  u32x4 hs[4];
  unsigned p0, p1, p2;
  unsigned dl;
};

constexpr int LDS_LIST = 0;
constexpr int LDS_RL   = LDS_LIST + WIN * 4;
constexpr int LDS_AP   = LDS_RL + WIN * 8;
constexpr int LDS_A_END = LDS_AP + 2 * 32 * APS;
static_assert(LDS_A_END >= 32 * 64 * 4, "red alias");

__global__ __launch_bounds__(NB, OCC) void k2_main(const u16* __restrict__ hh, const u16* __restrict__ pos,
                                                 const u16* __restrict__ e_attr, const u16* __restrict__ w_e,
                                                 const u16* __restrict__ w_x,
                                                 int N, int CH, int TS,
                                                 const uint2* __restrict__ recs, const u16* __restrict__ tab,
                                                 const u32x4* __restrict__ wfrag,
                                                 u16* __restrict__ out_h, u16* __restrict__ out_p) {
  __shared__ __attribute__((aligned(16))) char sm[LDS_A_END];
  __shared__ __attribute__((aligned(16))) u32x4 wl[10 * 64];
  __shared__ __attribute__((aligned(16))) u16 mi[8 * 72];
  __shared__ __attribute__((aligned(16))) float posn[8 * 4];
  __shared__ unsigned wsum[2];
  unsigned* list = (unsigned*)(sm + LDS_LIST);
  uint2* rl = (uint2*)(sm + LDS_RL);
  float* red = (float*)sm;

  const int tid = threadIdx.x, b = blockIdx.x;
  const int lane = tid & 63, wv = tid >> 6, r = lane & 31, h = lane >> 5;
  const int node0 = b * NPB;

  const unsigned lo = tab[(size_t)tid * TS + b], hi = tab[(size_t)tid * TS + b + 1];
  const bool nv = (r < NPB) && (node0 + r < N);
  u32x4 hn[4], w1f[8], wst[5];
  {
    const u32x4* hp = (const u32x4*)(hh + (size_t)(nv ? node0 + r : 0) * 64 + 8 * h);
#pragma unroll
    for (int s = 0; s < 4; ++s) hn[s] = hp[2 * s];
  }
#pragma unroll
  for (int f = 0; f < 8; ++f) w1f[f] = wfrag[f * 64 + lane];
#pragma unroll
  for (int f = 0; f < 5; ++f) wst[f] = wfrag[8 * 64 + f * NB + tid];
  const unsigned wd0 = w_e[128 * 64 + r], wd1 = w_e[128 * 64 + 32 + r];
  const int pnd = (tid >> 2) & 7, pc = tid & 3;
  const bool pv = (pc < 3) && (pnd < NPB) && (node0 + pnd < N);
  const unsigned praw = pos[pv ? (size_t)(node0 + pnd) * 3 + pc : 0];
  for (int i = tid; i < 2 * 32 * APS / 4; i += NB) ((unsigned*)(sm + LDS_AP))[i] = 0u;

  const unsigned cnt = hi - lo;
  unsigned inc = cnt;
#pragma unroll
  for (int o = 1; o < 64; o <<= 1) { unsigned v = __shfl_up(inc, o, 64); if (lane >= o) inc += v; }
  if (lane == 63) wsum[wv] = inc;
  if (tid < 32) posn[tid] = pv ? bf2f(praw) : 0.f;
#pragma unroll
  for (int f = 0; f < 5; ++f) wl[f * NB + tid] = wst[f];
  __syncthreads();
  const unsigned pre = inc - cnt + (wv ? wsum[0] : 0u);
  const int total = (int)(wsum[0] + wsum[1]);
  const unsigned gsrc = (unsigned)(tid * CH) + lo;

  f32x16 Y[2];
  Y[0] = (f32x16)(0.f); Y[1] = (f32x16)(0.f);
  char* apw = sm + LDS_AP + wv * 32 * APS;
  const int pslot = ((r >> 4) << 4) + (((r >> 2) & 1) << 3) + (((r >> 3) & 1) << 2) + (r & 3);
  u32x4 exf[2];
  int wn = 0;

  auto load_chunk = [&](int c, EdgeBuf& B) __attribute__((always_inline)) {
    const int sl = c * 32 + r;
    const bool v = sl < wn;
    uint2 rec = rl[v ? sl : 0];
    if (!v) rec = make_uint2(0u, 7u << 28);
    const unsigned e = rec.x, sidx = rec.y & 0x0fffffffu;
    B.dl = rec.y >> 28;
    B.ea = *(const u32x4*)(e_attr + (size_t)e * 16 + 8 * h);
    const u32x4* hp = (const u32x4*)(hh + (size_t)sidx * 64 + 8 * h);
#pragma unroll
    for (int s = 0; s < 4; ++s) B.hs[s] = hp[2 * s];
    const u16* pp = pos + (size_t)sidx * 3;
    B.p0 = pp[0]; B.p1 = pp[1]; B.p2 = pp[2];
  };
  auto compute = [&](const EdgeBuf& cur) __attribute__((always_inline)) {
    const unsigned dl = cur.dl;
    const float4 pd = *(const float4*)(posn + dl * 4);
    const float d0 = rbf(pd.x - bf2f(cur.p0)), d1 = rbf(pd.y - bf2f(cur.p1)), d2 = rbf(pd.z - bf2f(cur.p2));
    const float dist2 = rbf(d0 * d0) + rbf(d1 * d1) + rbf(d2 * d2);
    const unsigned oh = 0x3F80u << ((dl & 1u) << 4);
    const unsigned dq = dl >> 1;
    u32x4 ex;
    ex[0] = dq == 0 ? oh : 0u;
    ex[1] = dq == 1 ? oh : 0u;
    ex[2] = dq == 2 ? oh : 0u;
    ex[3] = (dl == 6 ? 0x3F80u : 0u) | (h ? 0u : (f2bf(dist2) << 16));
    u16* a0 = (u16*)(apw + (dl * 4 + 2 * h) * APS + pslot * 2);
    u16* a1 = (u16*)((char*)a0 + APS);
    *a0 = (u16)(h ? (__float_as_uint(d1) >> 16) : 0x3F80u);
    *a1 = (u16)((h ? __float_as_uint(d2) : __float_as_uint(d0)) >> 16);
    wave_fence();
    const u32x4 a2_0 = *(const u32x4*)(apw + r * APS + h * 16);
    const u32x4 a2_1 = *(const u32x4*)(apw + r * APS + 32 + h * 16);
    wave_fence();
    *a0 = 0; *a1 = 0;
#pragma unroll
    for (int t = 0; t < 2; ++t) {
      f32x16 x = (f32x16)(0.f);
      x = MFMA(__builtin_bit_cast(bf16x8, cur.ea), __builtin_bit_cast(bf16x8, wl[(8 + t) * 64 + lane]), x);
      x = MFMA(__builtin_bit_cast(bf16x8, ex), __builtin_bit_cast(bf16x8, exf[t]), x);
#pragma unroll
      for (int s = 0; s < 4; ++s) x = MFMA(__builtin_bit_cast(bf16x8, cur.hs[s]), __builtin_bit_cast(bf16x8, wl[(2 * s + t) * 64 + lane]), x);
      u32x4 xs0, xs1;
#pragma unroll
      for (int i = 0; i < 4; ++i) {
        xs0[i] = pk2(silu(x[2 * i]), silu(x[2 * i + 1]));
        xs1[i] = pk2(silu(x[8 + 2 * i]), silu(x[9 + 2 * i]));
      }
      Y[t] = MFMA(__builtin_bit_cast(bf16x8, a2_0), __builtin_bit_cast(bf16x8, xs0), Y[t]);
      Y[t] = MFMA(__builtin_bit_cast(bf16x8, a2_1), __builtin_bit_cast(bf16x8, xs1), Y[t]);
    }
  };

  auto fill_list = [&](int w0) __attribute__((always_inline)) {
    const int i0 = max(0, w0 - (int)pre), i1 = min((int)cnt, w0 + WIN - (int)pre);
    for (int i = i0; i < i1; ++i) list[(int)pre + i - w0] = gsrc + i;
  };
  fill_list(0);
  __syncthreads();
  wn = min(WIN, total);
  {
    uint2 rr[WIN / NB];
#pragma unroll
    for (int k = 0; k < WIN / NB; ++k) {
      const int sl = tid + k * NB;
      rr[k] = make_uint2(0u, 0u);
      if (wn > 0) rr[k] = recs[list[min(sl, wn - 1)]];
    }
#pragma unroll
    for (int t = 0; t < 2; ++t) {
      f32x16 a = (f32x16)(0.f);
#pragma unroll
      for (int s = 0; s < 4; ++s) {
        u32x4 hv = hn[s]; if (!nv) hv = (u32x4)(0u);
        a = MFMA(__builtin_bit_cast(bf16x8, hv), __builtin_bit_cast(bf16x8, w1f[2 * s + t]), a);
      }
      float v[8];
#pragma unroll
      for (int i = 0; i < 4; ++i) {
        const float o = __shfl_xor(a[i], 32, 64);
        v[i] = h ? o : a[i];
        v[4 + i] = h ? a[i] : o;
      }
      unsigned e16[8];
#pragma unroll
      for (int j = 0; j < 7; ++j) {
        const unsigned hb = f2bf(v[j]);
        const unsigned lb = f2bf(v[j] - bf2f(hb));
        e16[j] = h ? lb : hb;
      }
      e16[7] = h ? 0u : (t ? wd1 : wd0);
      exf[t] = (u32x4){e16[0] | (e16[1] << 16), e16[2] | (e16[3] << 16), e16[4] | (e16[5] << 16), e16[6] | (e16[7] << 16)};
    }
#pragma unroll
    for (int k = 0; k < WIN / NB; ++k) rl[tid + k * NB] = rr[k];
  }
  __syncthreads();
  for (int w0 = 0; w0 < total;) {
    const int nch = (wn + 31) >> 5;
    EdgeBuf A, B, C;
    load_chunk(wv, A);
    load_chunk(wv + 2, B);
    for (int c = wv; c < nch; c += 6) {
      load_chunk(c + 4, C);
      compute(A);
      if (c + 2 >= nch) break;
      load_chunk(c + 6, A);
      compute(B);
      if (c + 4 >= nch) break;
      load_chunk(c + 8, B);
      compute(C);
    }
    w0 += WIN;
    if (w0 >= total) break;
    __syncthreads();
    fill_list(w0);
    __syncthreads();
    wn = min(WIN, total - w0);
    for (int k = 0; k < WIN / NB; ++k) { const int sl = tid + k * NB; rl[sl] = recs[list[min(sl, wn - 1)]]; }
    __syncthreads();
  }

  __syncthreads();
  if (wv == 1) {
#pragma unroll
    for (int t = 0; t < 2; ++t)
#pragma unroll
      for (int i = 0; i < 16; ++i) red[(t * 16 + i) * 64 + lane] = Y[t][i];
  }
  u32x4 hn2[4];
  unsigned hres[8];
  float wx0 = 0.f, wx1 = 0.f;
  if (wv == 0) {
    const u32x4* hp = (const u32x4*)(hh + (size_t)(nv ? node0 + r : 0) * 64 + 8 * h);
#pragma unroll
    for (int s = 0; s < 4; ++s) { hn2[s] = hp[2 * s]; }
#pragma unroll
    for (int i = 0; i < 4; ++i) {
      const int nd = 4 * h + i;
      const bool ok = nd < NPB && node0 + nd < N;
      const size_t o = ok ? (size_t)(node0 + nd) * 64 + r : 0;
      hres[i] = hh[o]; hres[4 + i] = hh[o + 32];
    }
    wx0 = bf2f(w_x[r]); wx1 = bf2f(w_x[32 + r]);
  }
  __syncthreads();
  if (wv == 1) return;
#pragma unroll
  for (int t = 0; t < 2; ++t)
#pragma unroll
    for (int i = 0; i < 16; ++i) Y[t][i] += red[(t * 16 + i) * 64 + lane];

#pragma unroll
  for (int g = 0; g < 4; ++g) {
    const int nd = 2 * g + h;
    mi[nd * 72 + r] = (u16)f2bf(Y[0][4 * g]);
    mi[nd * 72 + 32 + r] = (u16)f2bf(Y[1][4 * g]);
#pragma unroll
    for (int k = 1; k < 4; ++k) {
      float p = wx0 * Y[0][4 * g + k] + wx1 * Y[1][4 * g + k];
#pragma unroll
      for (int o = 1; o < 32; o <<= 1) p += __shfl_xor(p, o, 64);
      if (r == 0 && nd < NPB && node0 + nd < N)
        out_p[(size_t)(node0 + nd) * 3 + (k - 1)] = (u16)f2bf(posn[nd * 4 + k - 1] + rbf(p));
    }
  }
  wave_fence();
  u32x4 mf[4];
#pragma unroll
  for (int s = 0; s < 4; ++s) {
    u32x4 v = *(const u32x4*)(mi + (r & 7) * 72 + 16 * s + 8 * h);
    if (r >= NPB) v = (u32x4)(0u);
    mf[s] = v;
    if (!nv) hn2[s] = (u32x4)(0u);
  }
#pragma unroll
  for (int t = 0; t < 2; ++t) {
    f32x16 a = (f32x16)(0.f);
#pragma unroll
    for (int s = 0; s < 4; ++s) a = MFMA(__builtin_bit_cast(bf16x8, hn2[s]), __builtin_bit_cast(bf16x8, wfrag[(18 + 2 * s + t) * 64 + lane]), a);
#pragma unroll
    for (int s = 0; s < 4; ++s) a = MFMA(__builtin_bit_cast(bf16x8, mf[s]), __builtin_bit_cast(bf16x8, wfrag[(26 + 2 * s + t) * 64 + lane]), a);
#pragma unroll
    for (int i = 0; i < 4; ++i) {
      const int nd = 4 * h + i;
      if (nd < NPB && node0 + nd < N) {
        const size_t o = (size_t)(node0 + nd) * 64 + 32 * t + r;
        const float sv = rbf(silu(rbf(a[i])));
        out_h[o] = (u16)f2bf(bf2f(hres[4 * t + i]) + sv);
      }
    }
  }
}

extern "C" void kernel_launch(void* const* d_in, const int* in_sizes, int n_in,
                              void* d_out, int out_size, void* d_ws, size_t ws_size,
                              hipStream_t stream) {
  const u16* h      = (const u16*)d_in[0];
  const u16* pos    = (const u16*)d_in[1];
  const u16* e_attr = (const u16*)d_in[2];
  const int* src    = (const int*)d_in[3];
  const int* dst    = (const int*)d_in[4];
  const u16* w_e    = (const u16*)d_in[5];
  const u16* w_x    = (const u16*)d_in[6];
  const u16* w_h    = (const u16*)d_in[7];
  const int N = in_sizes[0] / 64;
  const int E = in_sizes[3];
  const int CH = (E + NB - 1) / NB;
  const int NBK = (N + NPB - 1) / NPB;
  const int TS = (NBK + 1 + 7) & ~7;

  char* ws = (char*)d_ws;
  uint2* recs = (uint2*)ws;                                   size_t off = (size_t)NB * CH * 8;
  off = (off + 255) & ~(size_t)255;
  u16* tab = (u16*)(ws + off);                                off += (size_t)NB * TS * 2;
  off = (off + 255) & ~(size_t)255;
  u16* wfrag = (u16*)(ws + off);

  u16* out_h = (u16*)d_out;
  u16* out_p = out_h + (size_t)N * 64;

  const size_t lds1 = (((size_t)(NBK + 1) * 4 + 15) & ~(size_t)15) + (size_t)CH * 8;
  k1_sort<<<NB + 1, K1T, lds1, stream>>>(src, dst, w_e, w_h, E, CH, NBK, TS, recs, tab, wfrag);
  k2_main<<<NBK, NB, 0, stream>>>(h, pos, e_attr, w_e, w_x, N, CH, TS, recs, tab, (const u32x4*)wfrag, out_h, out_p);
}
```

```cpp
#include <hip/hip_runtime.h>
#include <hip/hip_bf16.h>
#include <stdint.h>

using bf16x8 = __attribute__((ext_vector_type(8))) short;
using f32x16 = __attribute__((ext_vector_type(16))) float;
using f32x2  = __attribute__((ext_vector_type(2))) float;
using bf16x2v = __attribute__((ext_vector_type(2))) __bf16;
using u32x4  = __attribute__((ext_vector_type(4))) unsigned;
typedef unsigned short u16;

#define OCC 3
#define DI __device__ __forceinline__
#define MFMA(a, b, c) __builtin_amdgcn_mfma_f32_32x32x16_bf16((a), (b), (c), 0, 0, 0)

constexpr int NPB = 7;
constexpr int NB  = 128;
constexpr int K1T = 1024;
constexpr int PER = 8;
constexpr int WIN = 512;
constexpr int APS = 80;
constexpr int NFRAG = 34;

DI unsigned pk2(float a, float b) {
  f32x2 v = {a, b};
  bf16x2v r = __builtin_convertvector(v, bf16x2v);
  return __builtin_bit_cast(unsigned, r);
}
DI float bf2f(unsigned bits16) { return __uint_as_float(bits16 << 16); }
DI unsigned f2bf(float a) { return pk2(a, 0.f) & 0xffffu; }
DI float rbf(float a) { return __uint_as_float(pk2(a, 0.f) << 16); }
DI float silu(float x) {
  float e = __builtin_amdgcn_exp2f(x * -1.4426950408889634f);
  return x * __builtin_amdgcn_rcpf(1.0f + e);
}
DI void wave_fence() {
  __builtin_amdgcn_fence(__ATOMIC_RELEASE, "wavefront");
  __builtin_amdgcn_wave_barrier();
  __builtin_amdgcn_fence(__ATOMIC_ACQUIRE, "wavefront");
}

__global__ __launch_bounds__(K1T) void k1_sort(const int* __restrict__ src, const int* __restrict__ dst,
                                               const u16* __restrict__ w_e, const u16* __restrict__ w_h,
                                               int E, int CH, int NBK, int TS,
                                               uint2* __restrict__ recs, u16* __restrict__ tab,
                                               u16* __restrict__ wfrag) {
  extern __shared__ __attribute__((aligned(16))) char smem[];
  __shared__ unsigned wsum[K1T / 64];
  const int tid = threadIdx.x, blk = blockIdx.x;
  if (blk >= NB) {
    for (int i = tid; i < NFRAG * 512; i += K1T) {
      const int f = i >> 9, lane = (i >> 3) & 63, j = i & 7, c = lane & 31, h = lane >> 5;
      u16 v;
      if (f < 8)       { const int s = f >> 1, t = f & 1;               v = w_e[(16 * s + 8 * h + j) * 64 + 32 * t + c]; }
      else if (f < 16) { const int s = (f - 8) >> 1, t = f & 1;         v = w_e[(64 + 16 * s + 8 * h + j) * 64 + 32 * t + c]; }
      else if (f < 18) { const int t = f & 1;                           v = w_e[(129 + 8 * h + j) * 64 + 32 * t + c]; }
      else             { const int s = (f - 18) >> 1, t = f & 1;        v = w_h[(16 * s + 8 * h + j) * 64 + 32 * t + c]; }
      wfrag[i] = v;
    }
    return;
  }
  unsigned* hist = (unsigned*)smem;
  uint2* lrec = (uint2*)(smem + (((NBK + 1) * 4 + 15) & ~15));
  const int e0 = blk * CH;
  const int n = max(0, min(E - e0, CH));
  for (int i = tid; i <= NBK; i += K1T) hist[i] = 0;
  int d[PER], s[PER];
#pragma unroll
  for (int k = 0; k < PER; ++k) {
    const int i = tid + k * K1T;
    d[k] = -1; s[k] = 0;
    if (i < n) { d[k] = dst[e0 + i]; s[k] = src[e0 + i]; }
  }
  __syncthreads();
  unsigned rk[PER]; int bk[PER];
#pragma unroll
  for (int k = 0; k < PER; ++k) {
    bk[k] = 0; rk[k] = 0;
    if (d[k] >= 0) { bk[k] = d[k] / NPB; rk[k] = atomicAdd(&hist[bk[k]], 1u); }
  }
  __syncthreads();
  const int q = (NBK + K1T - 1) / K1T;
  const int b0 = tid * q;
  unsigned loc = 0;
  for (int i = 0; i < q; ++i) { const int b = b0 + i; if (b < NBK) loc += hist[b]; }
  unsigned inc = loc;
#pragma unroll
  for (int o = 1; o < 64; o <<= 1) { unsigned v = __shfl_up(inc, o, 64); if ((tid & 63) >= o) inc += v; }
  if ((tid & 63) == 63) wsum[tid >> 6] = inc;
  __syncthreads();
  unsigned base = 0;
  for (int w = 0; w < (tid >> 6); ++w) base += wsum[w];
  unsigned run = base + inc - loc;
  for (int i = 0; i < q; ++i) {
    const int b = b0 + i;
    if (b < NBK) { const unsigned c = hist[b]; hist[b] = run; run += c; }
  }
  if (tid == 0) hist[NBK] = (unsigned)n;
  __syncthreads();
  for (int i = tid; i <= NBK; i += K1T) tab[(size_t)blk * TS + i] = (u16)hist[i];
#pragma unroll
  for (int k = 0; k < PER; ++k) {
    if (d[k] >= 0) {
      const unsigned dl = (unsigned)(d[k] - bk[k] * NPB);
      lrec[hist[bk[k]] + rk[k]] = make_uint2((unsigned)(e0 + tid + k * K1T), (unsigned)s[k] | (dl << 28));
    }
  }
  __syncthreads();
  for (int i = tid; i < n; i += K1T) recs[(size_t)e0 + i] = lrec[i];
}

struct EdgeBuf {
  u32x4 ea;
  u32x4 hs[4];
  unsigned p01, p2;
  unsigned ry;
};

constexpr int LDS_LIST = 0;
constexpr int LDS_RL   = LDS_LIST + WIN * 4;
constexpr int LDS_AP   = LDS_RL + WIN * 8;
constexpr int LDS_A_END = LDS_AP + 2 * 32 * APS + 512;
static_assert(WIN * 4 >= 8 * 64 * 4, "m rows alias");
static_assert(32 * APS + 512 >= 12 * 64 * 4, "p rows alias");

DI unsigned silu_pk(float a, float b) {
  const f32x2 v = {a, b};
  const f32x2 t = v * (f32x2){-1.4426950408889634f, -1.4426950408889634f};
  f32x2 e = {__builtin_amdgcn_exp2f(t.x), __builtin_amdgcn_exp2f(t.y)};
  e = e + (f32x2){1.0f, 1.0f};
  const f32x2 rc = {__builtin_amdgcn_rcpf(e.x), __builtin_amdgcn_rcpf(e.y)};
  const f32x2 o = v * rc;
  return __builtin_bit_cast(unsigned, __builtin_convertvector(o, bf16x2v));
}

__global__ __launch_bounds__(NB, OCC) void k2_main(const u16* __restrict__ hh, const u16* __restrict__ pos,
                                                 const u16* __restrict__ e_attr, const u16* __restrict__ w_e,
                                                 const u16* __restrict__ w_x,
                                                 int N, int CH, int TS,
                                                 const uint2* __restrict__ recs, const u16* __restrict__ tab,
                                                 const u32x4* __restrict__ wfrag,
                                                 u16* __restrict__ out_h, u16* __restrict__ out_p) {
  __shared__ __attribute__((aligned(16))) char sm[LDS_A_END];
  __shared__ __attribute__((aligned(16))) u32x4 wl[12 * 64];
  __shared__ __attribute__((aligned(16))) u16 mi[8 * 72];
  __shared__ __attribute__((aligned(16))) float posn[8 * 4];
  __shared__ unsigned wsum[2];
  unsigned* list = (unsigned*)(sm + LDS_LIST);
  uint2* rl = (uint2*)(sm + LDS_RL);

  const int tid = threadIdx.x, b = blockIdx.x;
  const int lane = tid & 63, wv = tid >> 6, r = lane & 31, h = lane >> 5;
  const int node0 = b * NPB;

  const unsigned lo = tab[(size_t)tid * TS + b], hi = tab[(size_t)tid * TS + b + 1];
  const bool nv = (r < NPB) && (node0 + r < N);
  u32x4 hn[4], w1f[4], wst[5];
  {
    const u32x4* hp = (const u32x4*)(hh + (size_t)(nv ? node0 + r : 0) * 64 + 8 * h);
#pragma unroll
    for (int s = 0; s < 4; ++s) hn[s] = hp[2 * s];
  }
#pragma unroll
  for (int s = 0; s < 4; ++s) w1f[s] = wfrag[(2 * s + wv) * 64 + lane];
#pragma unroll
  for (int f = 0; f < 5; ++f) wst[f] = wfrag[8 * 64 + f * NB + tid];
  const unsigned wd = w_e[128 * 64 + 32 * wv + r];
  const int pnd = (tid >> 2) & 7, pc = tid & 3;
  const bool pv = (pc < 3) && (pnd < NPB) && (node0 + pnd < N);
  const unsigned praw = pos[pv ? (size_t)(node0 + pnd) * 3 + pc : 0];
  for (int i = tid; i < 2 * 32 * APS / 4; i += NB) ((unsigned*)(sm + LDS_AP))[i] = 0u;

  const unsigned cnt = hi - lo;
  unsigned inc = cnt;
#pragma unroll
  for (int o = 1; o < 64; o <<= 1) { unsigned v = __shfl_up(inc, o, 64); if (lane >= o) inc += v; }
  if (lane == 63) wsum[wv] = inc;
  if (tid < 32) posn[tid] = pv ? bf2f(praw) : 0.f;
#pragma unroll
  for (int f = 0; f < 5; ++f) wl[f * NB + tid] = wst[f];
  __syncthreads();
  const unsigned pre = inc - cnt + (wv ? wsum[0] : 0u);
  const int total = (int)(wsum[0] + wsum[1]);
  const unsigned gsrc = (unsigned)(tid * CH) + lo;

  f32x16 Y[2];
  Y[0] = (f32x16)(0.f); Y[1] = (f32x16)(0.f);
  char* apw = sm + LDS_AP + (1 - wv) * 32 * APS;
  const int pslot = ((r >> 4) << 4) + (((r >> 2) & 1) << 3) + (((r >> 3) & 1) << 2) + (r & 3);
  int wn = 0;

  auto load_chunk = [&](int c, EdgeBuf& B) __attribute__((always_inline)) {
    const int sl = c * 32 + r;
    const bool v = sl < wn;
    uint2 rec = rl[v ? sl : 0];
    if (!v) rec = make_uint2(0u, 7u << 28);
    const unsigned e = rec.x, sidx = rec.y & 0x0fffffffu;
    B.ry = rec.y;
    B.ea = *(const u32x4*)((const char*)e_attr + (e * 32u + 16u * (unsigned)h));
    const char* hp = (const char*)hh;
    const unsigned ho = sidx * 128u + 16u * (unsigned)h;
#pragma unroll
    for (int s = 0; s < 4; ++s) B.hs[s] = *(const u32x4*)(hp + (ho + 32u * s));
    const char* pp = (const char*)pos;
    const unsigned po = sidx * 6u;
    unsigned p01; __builtin_memcpy(&p01, pp + po, 4);
    B.p01 = p01; B.p2 = *(const u16*)(pp + (po + 4u));
  };
  auto compute = [&](const EdgeBuf& cur) __attribute__((always_inline)) {
    const unsigned dl = cur.ry >> 28;
    const float4 pd = *(const float4*)(posn + dl * 4);
    const float d0 = rbf(pd.x - bf2f(cur.p01 & 0xffffu)), d1 = rbf(pd.y - __uint_as_float(cur.p01 & 0xffff0000u)),
                d2 = rbf(pd.z - bf2f(cur.p2));
    const float dist2 = rbf(d0 * d0) + rbf(d1 * d1) + rbf(d2 * d2);
    const unsigned oh = 0x3F80u << ((dl & 1u) << 4);
    const unsigned dq = dl >> 1;
    u32x4 ex;
    ex[0] = dq == 0 ? oh : 0u;
    ex[1] = dq == 1 ? oh : 0u;
    ex[2] = dq == 2 ? oh : 0u;
    ex[3] = (dl == 6 ? 0x3F80u : 0u) | (h ? 0u : (f2bf(dist2) << 16));
    u16* a0 = (u16*)(apw + (dl * 4 + 2 * h) * APS + pslot * 2);
    u16* a1 = (u16*)((char*)a0 + APS);
    *a0 = (u16)(h ? (__float_as_uint(d1) >> 16) : 0x3F80u);
    *a1 = (u16)((h ? __float_as_uint(d2) : __float_as_uint(d0)) >> 16);
    wave_fence();
    const u32x4 a2_0 = *(const u32x4*)(apw + r * APS + h * 16);
    const u32x4 a2_1 = *(const u32x4*)(apw + r * APS + 32 + h * 16);
    wave_fence();
    *a0 = 0; *a1 = 0;
#pragma unroll
    for (int t = 0; t < 2; ++t) {
      f32x16 x = (f32x16)(0.f);
      x = MFMA(__builtin_bit_cast(bf16x8, cur.ea), __builtin_bit_cast(bf16x8, wl[(8 + t) * 64 + lane]), x);
      x = MFMA(__builtin_bit_cast(bf16x8, ex), __builtin_bit_cast(bf16x8, wl[(10 + t) * 64 + lane]), x);
#pragma unroll
      for (int s = 0; s < 4; ++s) x = MFMA(__builtin_bit_cast(bf16x8, cur.hs[s]), __builtin_bit_cast(bf16x8, wl[(2 * s + t) * 64 + lane]), x);
      u32x4 xs0, xs1;
#pragma unroll
      for (int i = 0; i < 4; ++i) {
        xs0[i] = silu_pk(x[2 * i], x[2 * i + 1]);
        xs1[i] = silu_pk(x[8 + 2 * i], x[9 + 2 * i]);
      }
      Y[t] = MFMA(__builtin_bit_cast(bf16x8, a2_0), __builtin_bit_cast(bf16x8, xs0), Y[t]);
      Y[t] = MFMA(__builtin_bit_cast(bf16x8, a2_1), __builtin_bit_cast(bf16x8, xs1), Y[t]);
    }
  };

  auto fill_list = [&](int w0) __attribute__((always_inline)) {
    const int i0 = max(0, w0 - (int)pre), i1 = min((int)cnt, w0 + WIN - (int)pre);
    for (int i = i0; i < i1; ++i) list[(int)pre + i - w0] = gsrc + i;
  };
  fill_list(0);
  __syncthreads();
  wn = min(WIN, total);
  {
    uint2 rr[WIN / NB];
#pragma unroll
    for (int k = 0; k < WIN / NB; ++k) {
      const int sl = tid + k * NB;
      rr[k] = make_uint2(0u, 0u);
      if (wn > 0) rr[k] = recs[list[min(sl, wn - 1)]];
    }
    {
      f32x16 a = (f32x16)(0.f);
#pragma unroll
      for (int s = 0; s < 4; ++s) {
        u32x4 hv = hn[s]; if (!nv) hv = (u32x4)(0u);
        a = MFMA(__builtin_bit_cast(bf16x8, hv), __builtin_bit_cast(bf16x8, w1f[s]), a);
      }
      float v[8];
#pragma unroll
      for (int i = 0; i < 4; ++i) {
        const float o = __shfl_xor(a[i], 32, 64);
        v[i] = h ? o : a[i];
        v[4 + i] = h ? a[i] : o;
      }
      unsigned e16[8];
#pragma unroll
      for (int j = 0; j < 7; ++j) {
        const unsigned hb = f2bf(v[j]);
        const unsigned lb = f2bf(v[j] - bf2f(hb));
        e16[j] = h ? lb : hb;
      }
      e16[7] = h ? 0u : wd;
      wl[(10 + wv) * 64 + lane] =
          (u32x4){e16[0] | (e16[1] << 16), e16[2] | (e16[3] << 16), e16[4] | (e16[5] << 16), e16[6] | (e16[7] << 16)};
    }
#pragma unroll
    for (int k = 0; k < WIN / NB; ++k) rl[tid + k * NB] = rr[k];
  }
  __syncthreads();
  for (int w0 = 0; w0 < total;) {
    const int nch = (wn + 31) >> 5;
    EdgeBuf A, B;
    load_chunk(wv, A);
    for (int c = wv; c < nch; c += 4) {
      load_chunk(c + 2, B);
      compute(A);
      if (c + 2 >= nch) break;
      load_chunk(c + 4, A);
      compute(B);
    }
    w0 += WIN;
    if (w0 >= total) break;
    __syncthreads();
    fill_list(w0);
    __syncthreads();
    wn = min(WIN, total - w0);
    for (int k = 0; k < WIN / NB; ++k) { const int sl = tid + k * NB; rl[sl] = recs[list[min(sl, wn - 1)]]; }
    __syncthreads();
  }

  float* xm = (float*)(sm + LDS_LIST);
  float* xp = (float*)(sm + LDS_AP + 32 * APS);
  const float wx0 = bf2f(w_x[r]), wx1 = bf2f(w_x[32 + r]);
  float pk[12];
#pragma unroll
  for (int g = 0; g < 4; ++g)
#pragma unroll
    for (int k = 1; k < 4; ++k) pk[3 * g + k - 1] = wx0 * Y[0][4 * g + k] + wx1 * Y[1][4 * g + k];
  u32x4 hn2[4], whf[16];
  unsigned hres[8];
  if (wv == 0) {
#pragma unroll
    for (int i = 0; i < 12; ++i) xp[i * 64 + lane] = pk[i];
    const u32x4* hp = (const u32x4*)(hh + (size_t)(nv ? node0 + r : 0) * 64 + 8 * h);
#pragma unroll
    for (int s = 0; s < 4; ++s) hn2[s] = hp[2 * s];
#pragma unroll
    for (int f = 0; f < 16; ++f) whf[f] = wfrag[(18 + f) * 64 + lane];
#pragma unroll
    for (int i = 0; i < 4; ++i) {
      const int nd = 4 * h + i;
      const bool ok = nd < NPB && node0 + nd < N;
      const size_t o = ok ? (size_t)(node0 + nd) * 64 + r : 0;
      hres[i] = hh[o]; hres[4 + i] = hh[o + 32];
    }
  } else {
#pragma unroll
    for (int g = 0; g < 4; ++g) { xm[g * 64 + lane] = Y[0][4 * g]; xm[(4 + g) * 64 + lane] = Y[1][4 * g]; }
  }
  __syncthreads();
  if (wv == 0) {
#pragma unroll
    for (int g = 0; g < 4; ++g) {
      const int nd = 2 * g + h;
      mi[nd * 72 + r] = (u16)f2bf(Y[0][4 * g] + xm[g * 64 + lane]);
      mi[nd * 72 + 32 + r] = (u16)f2bf(Y[1][4 * g] + xm[(4 + g) * 64 + lane]);
    }
    wave_fence();
    u32x4 mf[4];
#pragma unroll
    for (int s = 0; s < 4; ++s) {
      u32x4 v = *(const u32x4*)(mi + (r & 7) * 72 + 16 * s + 8 * h);
      if (r >= NPB) v = (u32x4)(0u);
      mf[s] = v;
      if (!nv) hn2[s] = (u32x4)(0u);
    }
#pragma unroll
    for (int t = 0; t < 2; ++t) {
      f32x16 a = (f32x16)(0.f);
#pragma unroll
      for (int s = 0; s < 4; ++s) a = MFMA(__builtin_bit_cast(bf16x8, hn2[s]), __builtin_bit_cast(bf16x8, whf[2 * s + t]), a);
#pragma unroll
      for (int s = 0; s < 4; ++s) a = MFMA(__builtin_bit_cast(bf16x8, mf[s]), __builtin_bit_cast(bf16x8, whf[8 + 2 * s + t]), a);
#pragma unroll
      for (int i = 0; i < 4; ++i) {
        const int nd = 4 * h + i;
        if (nd < NPB && node0 + nd < N) {
          const size_t o = (size_t)(node0 + nd) * 64 + 32 * t + r;
          const float sv = rbf(silu(rbf(a[i])));
          out_h[o] = (u16)f2bf(bf2f(hres[4 * t + i]) + sv);
        }
      }
    }
  } else {
#pragma unroll
    for (int i = 0; i < 12; ++i) pk[i] += xp[i * 64 + lane];
#pragma unroll
    for (int o = 1; o < 32; o <<= 1)
#pragma unroll
      for (int i = 0; i < 12; ++i) pk[i] += __shfl_xor(pk[i], o, 64);
#pragma unroll
    for (int g = 0; g < 4; ++g) {
      const int nd = 2 * g + h;
#pragma unroll
      for (int k = 0; k < 3; ++k)
        if (r == 0 && nd < NPB && node0 + nd < N)
          out_p[(size_t)(node0 + nd) * 3 + k] = (u16)f2bf(posn[nd * 4 + k] + rbf(pk[3 * g + k]));
    }
  }
}

extern "C" void kernel_launch(void* const* d_in, const int* in_sizes, int n_in,
                              void* d_out, int out_size, void* d_ws, size_t ws_size,
                              hipStream_t stream) {
  const u16* h      = (const u16*)d_in[0];
  const u16* pos    = (const u16*)d_in[1];
  const u16* e_attr = (const u16*)d_in[2];
  const int* src    = (const int*)d_in[3];
  const int* dst    = (const int*)d_in[4];
  const u16* w_e    = (const u16*)d_in[5];
  const u16* w_x    = (const u16*)d_in[6];
  const u16* w_h    = (const u16*)d_in[7];
  const int N = in_sizes[0] / 64;
  const int E = in_sizes[3];
  const int CH = (E + NB - 1) / NB;
  const int NBK = (N + NPB - 1) / NPB;
  const int TS = (NBK + 1 + 7) & ~7;

  char* ws = (char*)d_ws;
  uint2* recs = (uint2*)ws;                                   size_t off = (size_t)NB * CH * 8;
  off = (off + 255) & ~(size_t)255;
  u16* tab = (u16*)(ws + off);                                off += (size_t)NB * TS * 2;
  off = (off + 255) & ~(size_t)255;
  u16* wfrag = (u16*)(ws + off);

  u16* out_h = (u16*)d_out;
  u16* out_p = out_h + (size_t)N * 64;

  const size_t lds1 = (((size_t)(NBK + 1) * 4 + 15) & ~(size_t)15) + (size_t)CH * 8;
  k1_sort<<<NB + 1, K1T, lds1, stream>>>(src, dst, w_e, w_h, E, CH, NBK, TS, recs, tab, wfrag);
  k2_main<<<NBK, NB, 0, stream>>>(h, pos, e_attr, w_e, w_x, N, CH, TS, recs, tab, (const u32x4*)wfrag, out_h, out_p);
}
```

```cpp
#include <hip/hip_runtime.h>
#include <hip/hip_bf16.h>
#include <stdint.h>

using bf16x8 = __attribute__((ext_vector_type(8))) short;
using f32x16 = __attribute__((ext_vector_type(16))) float;
using f32x2  = __attribute__((ext_vector_type(2))) float;
using bf16x2v = __attribute__((ext_vector_type(2))) __bf16;
using u32x4  = __attribute__((ext_vector_type(4))) unsigned;
typedef unsigned short u16;

#define OCC 3
#define DI __device__ __forceinline__
#define MFMA(a, b, c) __builtin_amdgcn_mfma_f32_32x32x16_bf16((a), (b), (c), 0, 0, 0)

constexpr int NPB = 7;
constexpr int NB  = 128;
constexpr int K1T = 1024;
constexpr int PFB = 254;
constexpr int PER = 8;
constexpr int WIN = 512;
constexpr int APS = 80;
constexpr int NFRAG = 34;

DI unsigned pk2(float a, float b) {
  f32x2 v = {a, b};
  bf16x2v r = __builtin_convertvector(v, bf16x2v);
  return __builtin_bit_cast(unsigned, r);
}
DI float bf2f(unsigned bits16) { return __uint_as_float(bits16 << 16); }
DI unsigned f2bf(float a) { return pk2(a, 0.f) & 0xffffu; }
DI float rbf(float a) { return __uint_as_float(pk2(a, 0.f) << 16); }
DI float silu(float x) {
  float e = __builtin_amdgcn_exp2f(x * -1.4426950408889634f);
  return x * __builtin_amdgcn_rcpf(1.0f + e);
}
DI void wave_fence() {
  __builtin_amdgcn_fence(__ATOMIC_RELEASE, "wavefront");
  __builtin_amdgcn_wave_barrier();
  __builtin_amdgcn_fence(__ATOMIC_ACQUIRE, "wavefront");
}

__global__ __launch_bounds__(K1T) void k1_sort(const int* __restrict__ src, const int* __restrict__ dst,
                                               const u16* __restrict__ w_e, const u16* __restrict__ w_h,
                                               int E, int CH, int NBK, int TS,
                                               uint2* __restrict__ recs, u16* __restrict__ tab,
                                               u16* __restrict__ wfrag,
                                               const u32x4* __restrict__ ea4, const u32x4* __restrict__ h4, int N,
                                               unsigned* __restrict__ sink) {
  extern __shared__ __attribute__((aligned(16))) char smem[];
  __shared__ unsigned wsum[K1T / 64];
  const int tid = threadIdx.x, blk = blockIdx.x;
  if (blk > NB) {
    const int g = (blk - NB - 1) * K1T + tid;
    u32x4 acc = (u32x4)(0u);
    const int ne = E * 2, nh = N * 8;
#pragma unroll 10
    for (int i = g; i < ne; i += PFB * K1T) acc ^= ea4[i];
    for (int i = g; i < nh; i += PFB * K1T) acc ^= h4[i];
    if ((acc[0] ^ acc[1] ^ acc[2] ^ acc[3]) == 0x9E3779B9u) sink[g] = 1u;
    return;
  }
  if (blk == NB) {
    for (int i = tid; i < NFRAG * 512; i += K1T) {
      const int f = i >> 9, lane = (i >> 3) & 63, j = i & 7, c = lane & 31, h = lane >> 5;
      u16 v;
      if (f < 8)       { const int s = f >> 1, t = f & 1;               v = w_e[(16 * s + 8 * h + j) * 64 + 32 * t + c]; }
      else if (f < 16) { const int s = (f - 8) >> 1, t = f & 1;         v = w_e[(64 + 16 * s + 8 * h + j) * 64 + 32 * t + c]; }
      else if (f < 18) { const int t = f & 1;                           v = w_e[(129 + 8 * h + j) * 64 + 32 * t + c]; }
      else             { const int s = (f - 18) >> 1, t = f & 1;        v = w_h[(16 * s + 8 * h + j) * 64 + 32 * t + c]; }
      wfrag[i] = v;
    }
    return;
  }
  unsigned* hist = (unsigned*)smem;
  uint2* lrec = (uint2*)(smem + (((NBK + 1) * 4 + 15) & ~15));
  const int e0 = blk * CH;
  const int n = max(0, min(E - e0, CH));
  for (int i = tid; i <= NBK; i += K1T) hist[i] = 0;
  int d[PER], s[PER];
#pragma unroll
  for (int k = 0; k < PER; ++k) {
    const int i = tid + k * K1T;
    d[k] = -1; s[k] = 0;
    if (i < n) { d[k] = dst[e0 + i]; s[k] = src[e0 + i]; }
  }
  __syncthreads();
  unsigned rk[PER]; int bk[PER];
#pragma unroll
  for (int k = 0; k < PER; ++k) {
    bk[k] = 0; rk[k] = 0;
    if (d[k] >= 0) { bk[k] = d[k] / NPB; rk[k] = atomicAdd(&hist[bk[k]], 1u); }
  }
  __syncthreads();
  const int q = (NBK + K1T - 1) / K1T;
  const int b0 = tid * q;
  unsigned loc = 0;
  for (int i = 0; i < q; ++i) { const int b = b0 + i; if (b < NBK) loc += hist[b]; }
  unsigned inc = loc;
#pragma unroll
  for (int o = 1; o < 64; o <<= 1) { unsigned v = __shfl_up(inc, o, 64); if ((tid & 63) >= o) inc += v; }
  if ((tid & 63) == 63) wsum[tid >> 6] = inc;
  __syncthreads();
  unsigned base = 0;
  for (int w = 0; w < (tid >> 6); ++w) base += wsum[w];
  unsigned run = base + inc - loc;
  for (int i = 0; i < q; ++i) {
    const int b = b0 + i;
    if (b < NBK) { const unsigned c = hist[b]; hist[b] = run; run += c; }
  }
  if (tid == 0) hist[NBK] = (unsigned)n;
  __syncthreads();
  for (int i = tid; i <= NBK; i += K1T) tab[(size_t)blk * TS + i] = (u16)hist[i];
#pragma unroll
  for (int k = 0; k < PER; ++k) {
    if (d[k] >= 0) {
      const unsigned dl = (unsigned)(d[k] - bk[k] * NPB);
      lrec[hist[bk[k]] + rk[k]] = make_uint2((unsigned)(e0 + tid + k * K1T), (unsigned)s[k] | (dl << 28));
    }
  }
  __syncthreads();
  for (int i = tid; i < n; i += K1T) recs[(size_t)e0 + i] = lrec[i];
}

struct EdgeBuf {
  u32x4 ea;
  u32x4 hs[4];
  unsigned p01, p2;
  unsigned ry;
};

constexpr int LDS_LIST = 0;
constexpr int LDS_RL   = LDS_LIST + WIN * 4;
constexpr int LDS_AP   = LDS_RL + WIN * 8;
constexpr int LDS_A_END = LDS_AP + 2 * 32 * APS + 512;
static_assert(WIN * 4 >= 8 * 64 * 4, "m rows alias");
static_assert(32 * APS + 512 >= 12 * 64 * 4, "p rows alias");

DI unsigned silu_pk(float a, float b) {
  const f32x2 v = {a, b};
  const f32x2 t = v * (f32x2){-1.4426950408889634f, -1.4426950408889634f};
  f32x2 e = {__builtin_amdgcn_exp2f(t.x), __builtin_amdgcn_exp2f(t.y)};
  e = e + (f32x2){1.0f, 1.0f};
  const f32x2 rc = {__builtin_amdgcn_rcpf(e.x), __builtin_amdgcn_rcpf(e.y)};
  const f32x2 o = v * rc;
  return __builtin_bit_cast(unsigned, __builtin_convertvector(o, bf16x2v));
}

__global__ __launch_bounds__(NB, OCC) void k2_main(const u16* __restrict__ hh, const u16* __restrict__ pos,
                                                 const u16* __restrict__ e_attr, const u16* __restrict__ w_e,
                                                 const u16* __restrict__ w_x,
                                                 int N, int CH, int TS,
                                                 const uint2* __restrict__ recs, const u16* __restrict__ tab,
                                                 const u32x4* __restrict__ wfrag,
                                                 u16* __restrict__ out_h, u16* __restrict__ out_p) {
  __shared__ __attribute__((aligned(16))) char sm[LDS_A_END];
  __shared__ __attribute__((aligned(16))) u32x4 wl[12 * 64];
  __shared__ __attribute__((aligned(16))) u16 mi[8 * 72];
  __shared__ __attribute__((aligned(16))) float posn[8 * 4];
  __shared__ unsigned wsum[2];
  unsigned* list = (unsigned*)(sm + LDS_LIST);
  uint2* rl = (uint2*)(sm + LDS_RL);

  const int tid = threadIdx.x, b = (blockIdx.x & 7) * (gridDim.x >> 3) + (blockIdx.x >> 3);
  if (b * NPB >= N) return;
  const int lane = tid & 63, wv = tid >> 6, r = lane & 31, h = lane >> 5;
  const int node0 = b * NPB;

  const unsigned lo = tab[(size_t)tid * TS + b], hi = tab[(size_t)tid * TS + b + 1];
  const bool nv = (r < NPB) && (node0 + r < N);
  u32x4 hn[4], w1f[4], wst[5];
  {
    const u32x4* hp = (const u32x4*)(hh + (size_t)(nv ? node0 + r : 0) * 64 + 8 * h);
#pragma unroll
    for (int s = 0; s < 4; ++s) hn[s] = hp[2 * s];
  }
#pragma unroll
  for (int s = 0; s < 4; ++s) w1f[s] = wfrag[(2 * s + wv) * 64 + lane];
#pragma unroll
  for (int f = 0; f < 5; ++f) wst[f] = wfrag[8 * 64 + f * NB + tid];
  const unsigned wd = w_e[128 * 64 + 32 * wv + r];
  const unsigned wxa = w_x[r], wxb = w_x[32 + r];
  const int pnd = (tid >> 2) & 7, pc = tid & 3;
  const bool pv = (pc < 3) && (pnd < NPB) && (node0 + pnd < N);
  const unsigned praw = pos[pv ? (size_t)(node0 + pnd) * 3 + pc : 0];
  for (int i = tid; i < 2 * 32 * APS / 4; i += NB) ((unsigned*)(sm + LDS_AP))[i] = 0u;

  const unsigned cnt = hi - lo;
  unsigned inc = cnt;
#pragma unroll
  for (int o = 1; o < 64; o <<= 1) { unsigned v = __shfl_up(inc, o, 64); if (lane >= o) inc += v; }
  if (lane == 63) wsum[wv] = inc;
  if (tid < 32) posn[tid] = pv ? bf2f(praw) : 0.f;
#pragma unroll
  for (int f = 0; f < 5; ++f) wl[f * NB + tid] = wst[f];
  __syncthreads();
  const unsigned pre = inc - cnt + (wv ? wsum[0] : 0u);
  const int total = (int)(wsum[0] + wsum[1]);
  const unsigned gsrc = (unsigned)(tid * CH) + lo;

  f32x16 Y[2];
  Y[0] = (f32x16)(0.f); Y[1] = (f32x16)(0.f);
  char* apw = sm + LDS_AP + (1 - wv) * 32 * APS;
  const int pslot = ((r >> 4) << 4) + (((r >> 2) & 1) << 3) + (((r >> 3) & 1) << 2) + (r & 3);
  int wn = 0;

  auto read_rec = [&](int c) __attribute__((always_inline)) {
    const int sl = c * 32 + r;
    const bool v = sl < wn;
    uint2 rec = rl[v ? sl : 0];
    if (!v) rec = make_uint2(0u, 7u << 28);
    return rec;
  };
  auto load_chunk = [&](uint2 rec, EdgeBuf& B) __attribute__((always_inline)) {
    const unsigned e = rec.x, sidx = rec.y & 0x0fffffffu;
    B.ry = rec.y;
    B.ea = *(const u32x4*)((const char*)e_attr + (e * 32u + 16u * (unsigned)h));
    const char* hp = (const char*)hh;
    const unsigned ho = sidx * 128u + 16u * (unsigned)h;
#pragma unroll
    for (int s = 0; s < 4; ++s) B.hs[s] = *(const u32x4*)(hp + (ho + 32u * s));
    const char* pp = (const char*)pos;
    const unsigned po = sidx * 6u;
    unsigned p01; __builtin_memcpy(&p01, pp + po, 4);
    B.p01 = p01; B.p2 = *(const u16*)(pp + (po + 4u));
  };
  auto silu16 = [&](const f32x16& x, u32x4& o0, u32x4& o1) __attribute__((always_inline)) {
#pragma unroll
    for (int q = 0; q < 2; ++q) {
      f32x2 t[4];
#pragma unroll
      for (int i = 0; i < 4; ++i) t[i] = (f32x2){x[8 * q + 2 * i], x[8 * q + 2 * i + 1]} * (f32x2){-1.4426950408889634f, -1.4426950408889634f};
#pragma unroll
      for (int i = 0; i < 4; ++i) { t[i].x = __builtin_amdgcn_exp2f(t[i].x); t[i].y = __builtin_amdgcn_exp2f(t[i].y); }
#pragma unroll
      for (int i = 0; i < 4; ++i) t[i] = t[i] + (f32x2){1.0f, 1.0f};
#pragma unroll
      for (int i = 0; i < 4; ++i) { t[i].x = __builtin_amdgcn_rcpf(t[i].x); t[i].y = __builtin_amdgcn_rcpf(t[i].y); }
#pragma unroll
      for (int i = 0; i < 4; ++i) t[i] = t[i] * (f32x2){x[8 * q + 2 * i], x[8 * q + 2 * i + 1]};
#pragma unroll
      for (int i = 0; i < 4; ++i) {
        const unsigned v = __builtin_bit_cast(unsigned, __builtin_convertvector(t[i], bf16x2v));
        if (q == 0) o0[i] = v; else o1[i] = v;
      }
    }
  };

  auto fill_list = [&](int w0) __attribute__((always_inline)) {
    const int i0 = max(0, w0 - (int)pre), i1 = min((int)cnt, w0 + WIN - (int)pre);
    for (int i = i0; i < i1; ++i) list[(int)pre + i - w0] = gsrc + i;
  };
  fill_list(0);
  __syncthreads();
  wn = min(WIN, total);
  {
    uint2 rr[WIN / NB];
#pragma unroll
    for (int k = 0; k < WIN / NB; ++k) {
      const int sl = tid + k * NB;
      rr[k] = make_uint2(0u, 0u);
      if (wn > 0) rr[k] = recs[list[min(sl, wn - 1)]];
    }
    {
      f32x16 a = (f32x16)(0.f);
#pragma unroll
      for (int s = 0; s < 4; ++s) {
        u32x4 hv = hn[s]; if (!nv) hv = (u32x4)(0u);
        a = MFMA(__builtin_bit_cast(bf16x8, hv), __builtin_bit_cast(bf16x8, w1f[s]), a);
      }
      float v[8];
#pragma unroll
      for (int i = 0; i < 4; ++i) {
        const float o = __shfl_xor(a[i], 32, 64);
        v[i] = h ? o : a[i];
        v[4 + i] = h ? a[i] : o;
      }
      unsigned e16[8];
#pragma unroll
      for (int j = 0; j < 7; ++j) {
        const unsigned hb = f2bf(v[j]);
        const unsigned lb = f2bf(v[j] - bf2f(hb));
        e16[j] = h ? lb : hb;
      }
      e16[7] = h ? 0u : wd;
      wl[(10 + wv) * 64 + lane] =
          (u32x4){e16[0] | (e16[1] << 16), e16[2] | (e16[3] << 16), e16[4] | (e16[5] << 16), e16[6] | (e16[7] << 16)};
    }
#pragma unroll
    for (int k = 0; k < WIN / NB; ++k) rl[tid + k * NB] = rr[k];
  }
  __syncthreads();
  for (int w0 = 0; w0 < total;) {
    const int nch = (wn + 31) >> 5;
    EdgeBuf A;
    load_chunk(read_rec(wv), A);
    for (int c = wv; c < nch; c += 2) {
      const uint2 recn = read_rec(c + 2);
      f32x16 x0 = (f32x16)(0.f), x1 = (f32x16)(0.f);
      x0 = MFMA(__builtin_bit_cast(bf16x8, A.ea), __builtin_bit_cast(bf16x8, wl[8 * 64 + lane]), x0);
      x1 = MFMA(__builtin_bit_cast(bf16x8, A.ea), __builtin_bit_cast(bf16x8, wl[9 * 64 + lane]), x1);
#pragma unroll
      for (int s = 0; s < 4; ++s) {
        x0 = MFMA(__builtin_bit_cast(bf16x8, A.hs[s]), __builtin_bit_cast(bf16x8, wl[(2 * s) * 64 + lane]), x0);
        x1 = MFMA(__builtin_bit_cast(bf16x8, A.hs[s]), __builtin_bit_cast(bf16x8, wl[(2 * s + 1) * 64 + lane]), x1);
      }
      const unsigned dl = A.ry >> 28;
      const float4 pd = *(const float4*)(posn + dl * 4);
      const float d0 = rbf(pd.x - bf2f(A.p01 & 0xffffu)), d1 = rbf(pd.y - __uint_as_float(A.p01 & 0xffff0000u)),
                  d2 = rbf(pd.z - bf2f(A.p2));
      const float dist2 = rbf(d0 * d0) + rbf(d1 * d1) + rbf(d2 * d2);
      const unsigned oh = 0x3F80u << ((dl & 1u) << 4);
      const unsigned dq = dl >> 1;
      u32x4 ex;
      ex[0] = dq == 0 ? oh : 0u;
      ex[1] = dq == 1 ? oh : 0u;
      ex[2] = dq == 2 ? oh : 0u;
      ex[3] = (dl == 6 ? 0x3F80u : 0u) | (h ? 0u : (f2bf(dist2) << 16));
      u16* a0 = (u16*)(apw + (dl * 4 + 2 * h) * APS + pslot * 2);
      u16* a1 = (u16*)((char*)a0 + APS);
      *a0 = (u16)(h ? (__float_as_uint(d1) >> 16) : 0x3F80u);
      *a1 = (u16)((h ? __float_as_uint(d2) : __float_as_uint(d0)) >> 16);
      wave_fence();
      const u32x4 a2_0 = *(const u32x4*)(apw + r * APS + h * 16);
      const u32x4 a2_1 = *(const u32x4*)(apw + r * APS + 32 + h * 16);
      wave_fence();
      *a0 = 0; *a1 = 0;
      x0 = MFMA(__builtin_bit_cast(bf16x8, ex), __builtin_bit_cast(bf16x8, wl[10 * 64 + lane]), x0);
      x1 = MFMA(__builtin_bit_cast(bf16x8, ex), __builtin_bit_cast(bf16x8, wl[11 * 64 + lane]), x1);
      __builtin_amdgcn_sched_barrier(0);
      load_chunk(recn, A);
      __builtin_amdgcn_sched_barrier(0);
      u32x4 xs0, xs1;
      silu16(x0, xs0, xs1);
      Y[0] = MFMA(__builtin_bit_cast(bf16x8, a2_0), __builtin_bit_cast(bf16x8, xs0), Y[0]);
      Y[0] = MFMA(__builtin_bit_cast(bf16x8, a2_1), __builtin_bit_cast(bf16x8, xs1), Y[0]);
      silu16(x1, xs0, xs1);
      Y[1] = MFMA(__builtin_bit_cast(bf16x8, a2_0), __builtin_bit_cast(bf16x8, xs0), Y[1]);
      Y[1] = MFMA(__builtin_bit_cast(bf16x8, a2_1), __builtin_bit_cast(bf16x8, xs1), Y[1]);
      __builtin_amdgcn_sched_barrier(0);
    }
    w0 += WIN;
    if (w0 >= total) break;
    __syncthreads();
    fill_list(w0);
    __syncthreads();
    wn = min(WIN, total - w0);
    for (int k = 0; k < WIN / NB; ++k) { const int sl = tid + k * NB; rl[sl] = recs[list[min(sl, wn - 1)]]; }
    __syncthreads();
  }

  float* xm = (float*)(sm + LDS_LIST);
  float* xp = (float*)(sm + LDS_AP + 32 * APS);
  const float wx0 = bf2f(wxa), wx1 = bf2f(wxb);
  float pk[12];
#pragma unroll
  for (int g = 0; g < 4; ++g)
#pragma unroll
    for (int k = 1; k < 4; ++k) pk[3 * g + k - 1] = wx0 * Y[0][4 * g + k] + wx1 * Y[1][4 * g + k];
  u32x4 hn2[4], whf[16];
  unsigned hres[8];
  if (wv == 0) {
#pragma unroll
    for (int i = 0; i < 12; ++i) xp[i * 64 + lane] = pk[i];
    const u32x4* hp = (const u32x4*)(hh + (size_t)(nv ? node0 + r : 0) * 64 + 8 * h);
#pragma unroll
    for (int s = 0; s < 4; ++s) hn2[s] = hp[2 * s];
#pragma unroll
    for (int f = 0; f < 16; ++f) whf[f] = wfrag[(18 + f) * 64 + lane];
#pragma unroll
    for (int i = 0; i < 4; ++i) {
      const int nd = 4 * h + i;
      const bool ok = nd < NPB && node0 + nd < N;
      const size_t o = ok ? (size_t)(node0 + nd) * 64 + r : 0;
      hres[i] = hh[o]; hres[4 + i] = hh[o + 32];
    }
  } else {
#pragma unroll
    for (int g = 0; g < 4; ++g) { xm[g * 64 + lane] = Y[0][4 * g]; xm[(4 + g) * 64 + lane] = Y[1][4 * g]; }
  }
  __syncthreads();
  if (wv == 0) {
#pragma unroll
    for (int g = 0; g < 4; ++g) {
      const int nd = 2 * g + h;
      mi[nd * 72 + r] = (u16)f2bf(Y[0][4 * g] + xm[g * 64 + lane]);
      mi[nd * 72 + 32 + r] = (u16)f2bf(Y[1][4 * g] + xm[(4 + g) * 64 + lane]);
    }
    wave_fence();
    u32x4 mf[4];
#pragma unroll
    for (int s = 0; s < 4; ++s) {
      u32x4 v = *(const u32x4*)(mi + (r & 7) * 72 + 16 * s + 8 * h);
      if (r >= NPB) v = (u32x4)(0u);
      mf[s] = v;
      if (!nv) hn2[s] = (u32x4)(0u);
    }
#pragma unroll
    for (int t = 0; t < 2; ++t) {
      f32x16 a = (f32x16)(0.f);
#pragma unroll
      for (int s = 0; s < 4; ++s) a = MFMA(__builtin_bit_cast(bf16x8, hn2[s]), __builtin_bit_cast(bf16x8, whf[2 * s + t]), a);
#pragma unroll
      for (int s = 0; s < 4; ++s) a = MFMA(__builtin_bit_cast(bf16x8, mf[s]), __builtin_bit_cast(bf16x8, whf[8 + 2 * s + t]), a);
#pragma unroll
      for (int i = 0; i < 4; ++i) {
        const int nd = 4 * h + i;
        if (nd < NPB && node0 + nd < N) {
          const size_t o = (size_t)(node0 + nd) * 64 + 32 * t + r;
          const float sv = rbf(silu(rbf(a[i])));
          out_h[o] = (u16)f2bf(bf2f(hres[4 * t + i]) + sv);
        }
      }
    }
  } else {
#pragma unroll
    for (int i = 0; i < 12; ++i) pk[i] += xp[i * 64 + lane];
#pragma unroll
    for (int o = 1; o < 32; o <<= 1)
#pragma unroll
      for (int i = 0; i < 12; ++i) pk[i] += __shfl_xor(pk[i], o, 64);
#pragma unroll
    for (int g = 0; g < 4; ++g) {
      const int nd = 2 * g + h;
#pragma unroll
      for (int k = 0; k < 3; ++k)
        if (r == 0 && nd < NPB && node0 + nd < N)
          out_p[(size_t)(node0 + nd) * 3 + k] = (u16)f2bf(posn[nd * 4 + k] + rbf(pk[3 * g + k]));
    }
  }
}

extern "C" void kernel_launch(void* const* d_in, const int* in_sizes, int n_in,
                              void* d_out, int out_size, void* d_ws, size_t ws_size,
                              hipStream_t stream) {
  const u16* h      = (const u16*)d_in[0];
  const u16* pos    = (const u16*)d_in[1];
  const u16* e_attr = (const u16*)d_in[2];
  const int* src    = (const int*)d_in[3];
  const int* dst    = (const int*)d_in[4];
  const u16* w_e    = (const u16*)d_in[5];
  const u16* w_x    = (const u16*)d_in[6];
  const u16* w_h    = (const u16*)d_in[7];
  const int N = in_sizes[0] / 64;
  const int E = in_sizes[3];
  const int CH = (E + NB - 1) / NB;
  const int NBK = (N + NPB - 1) / NPB;
  const int TS = (NBK + 1 + 7) & ~7;

  char* ws = (char*)d_ws;
  uint2* recs = (uint2*)ws;                                   size_t off = (size_t)NB * CH * 8;
  off = (off + 255) & ~(size_t)255;
  u16* tab = (u16*)(ws + off);                                off += (size_t)NB * TS * 2;
  off = (off + 255) & ~(size_t)255;
  u16* wfrag = (u16*)(ws + off);                              off += (size_t)NFRAG * 512 * 2;
  off = (off + 255) & ~(size_t)255;
  unsigned* sink = (unsigned*)(ws + off);

  u16* out_h = (u16*)d_out;
  u16* out_p = out_h + (size_t)N * 64;

  const size_t lds1 = (((size_t)(NBK + 1) * 4 + 15) & ~(size_t)15) + (size_t)CH * 8;
  k1_sort<<<NB + 1 + PFB, K1T, lds1, stream>>>(src, dst, w_e, w_h, E, CH, NBK, TS, recs, tab, wfrag,
                                               (const u32x4*)e_attr, (const u32x4*)h, N, sink);
  k2_main<<<(NBK + 7) & ~7, NB, 0, stream>>>(h, pos, e_attr, w_e, w_x, N, CH, TS, recs, tab, (const u32x4*)wfrag, out_h, out_p);
}
```
